```python
import torch

def _softmax(x, axis=-1):
    return torch.softmax(x, dim=axis)

def _gelu(x):
    return torch.nn.functional.gelu(x, approximate='tanh')

def _silu(x):
    return torch.nn.functional.silu(x)

def _sigmoid(x):
    return torch.sigmoid(x)

def _layernorm(x, axis=-1, eps=1e-05):
    m = x.mean(dim=axis, keepdim=True)
    v = x.var(dim=axis, keepdim=True, unbiased=False)
    return (x - m) / torch.sqrt(v + eps)

def _scatter_add(tgt, idx, src, axis=0):
    return tgt.clone().index_add_(axis, idx, src)

def _segment_sum(src, seg, n):
    out = torch.zeros((n,) + src.shape[1:], dtype=src.dtype)
    return out.index_add_(0, seg, src)

def _pad(x, pw):
    flat = tuple((v for pair in reversed(pw) for v in pair))
    return torch.nn.functional.pad(x, flat)
kernel_name = 'adaLN_cond__mha_block_cached__swiglu_ffn__decode'

def setup_inputs():

    _w = lambda a: (lambda t: t.to(torch.bfloat16) if t.is_floating_point() else t)(torch.as_tensor(a))

    def _A_setup():
        B, Sq, D = (8, 1, 512)
        H, Sk, Dh = (32, 4096, 128)
        return {'x': _w((lambda *s: torch.randn(*s, dtype=torch.bfloat16) * 0.1)(B, Sq, D)), 'k_cache': _w((lambda *s: torch.randn(*s, dtype=torch.bfloat16) * 0.8)(B, H, Sk, Dh)), 'v_cache': _w((lambda *s: torch.randn(*s, dtype=torch.bfloat16) * 1.6)(B, H, Sk, Dh)), 'wq': _w((lambda *s: torch.randn(*s, dtype=torch.bfloat16) * 0.8)(D, H * Dh) * D ** (-0.5)), 'wo': _w((lambda *s: torch.randn(*s, dtype=torch.bfloat16) * 1.6)(H * Dh, D) * (H * Dh) ** (-0.5))}
    _A_in = _A_setup()

    def _B_setup():
        B, S, D, F = (8, 1, 512, 1376)
        return {'x': _w((lambda *s: torch.randn(*s, dtype=torch.bfloat16) * 0.1)(B, S, D)), 'w_gate': _w((lambda *s: torch.randn(*s, dtype=torch.bfloat16) * 0.1)(D, F)), 'w_up': _w((lambda *s: torch.randn(*s, dtype=torch.bfloat16) * 0.1)(D, F)), 'w_down': _w((lambda *s: torch.randn(*s, dtype=torch.bfloat16) * 0.1)(F, D))}
    _B_in = _B_setup()
    return {'A_x': _A_in['x'], 'A_k_cache': _A_in['k_cache'], 'A_v_cache': _A_in['v_cache'], 'A_wq': _A_in['wq'], 'A_wo': _A_in['wo'], 'B_x': _B_in['x'], 'B_w_gate': _B_in['w_gate'], 'B_w_up': _B_in['w_up'], 'B_w_down': _B_in['w_down'], 'c': _w((lambda *s: torch.randn(*s, dtype=torch.bfloat16) * 0.8)(8, 512) * 8.0), 'w_mod': _w((lambda *s: torch.randn(*s, dtype=torch.bfloat16) * 0.8)(512, 6 * 512) * 512 ** (-0.5) * 8.0)}

def reference(A_x, A_k_cache, A_v_cache, A_wq, A_wo, B_x, B_w_gate, B_w_up, B_w_down, c, w_mod):

    def _A_ref(x, k_cache, v_cache, wq, wo):
        B, Sq, D = x.shape
        H, Dh = (32, 128)
        q = (lambda x, *d: x.permute(*d))((x @ wq).reshape(B, Sq, H, Dh), 0, 2, 1, 3)
        scores = torch.einsum('bhqd,bhkd->bhqk', q, k_cache) * Dh ** (-0.5)
        p = _softmax(scores, axis=-1)
        o = torch.einsum('bhqk,bhkd->bhqd', p, v_cache)
        return (lambda x, *d: x.permute(*d))(o, 0, 2, 1, 3).reshape(B, Sq, H * Dh) @ wo

    def _B_ref(x, w_gate, w_up, w_down):
        g = _silu(x @ w_gate)
        u = x @ w_up
        return g * u @ w_down
    x = A_x
    mod = (_silu(c) @ w_mod).reshape(c.shape[0], 6, 1, -1)
    sa, ca, ga, sb, cb, gb = (mod[:, 0].clone(), mod[:, 1].clone(), mod[:, 2].clone(), mod[:, 3].clone(), mod[:, 4].clone(), mod[:, 5].clone())
    h = _layernorm(x, axis=-1) * (1 + ca) + sa
    x = x + ga * _A_ref(h, A_k_cache, A_v_cache, A_wq, A_wo)
    h = _layernorm(x, axis=-1) * (1 + cb) + sb
    x = x + gb * _B_ref(h, B_w_gate, B_w_up, B_w_down)
    return x
```

```cpp
#include <hip/hip_runtime.h>
#include <hip/hip_bf16.h>
#include <stdint.h>

#define MIMIC 1
#define MIMIC_SCORE 0

typedef unsigned short u16;
using u32x4 = __attribute__((ext_vector_type(4))) unsigned;
using bf16x2 = __attribute__((ext_vector_type(2))) __bf16;

constexpr int NB = 8, D = 512, H = 32, SK = 4096, DH = 128, F = 1376;

__device__ __forceinline__ float bf2f(u16 v) { return __uint_as_float(((unsigned)v) << 16); }
__device__ __forceinline__ u16 f2bf(float f) { return __builtin_bit_cast(u16, (__bf16)f); }
__device__ __forceinline__ float rbf(float f) {
  return bf2f(f2bf(f));
}
__device__ __forceinline__ float rbfs(float f) {
  return f;
}
__device__ __forceinline__ float lo16(unsigned x) { return __uint_as_float(x << 16); }
__device__ __forceinline__ float hi16(unsigned x) { return __uint_as_float(x & 0xffff0000u); }
__device__ __forceinline__ float dot2(unsigned k, unsigned q, float acc) {
  return __builtin_amdgcn_fdot2_f32_bf16(__builtin_bit_cast(bf16x2, k), __builtin_bit_cast(bf16x2, q), acc, false);
}
template <int CTRL> __device__ __forceinline__ float dpp(float x) {
  return __builtin_bit_cast(float, __builtin_amdgcn_mov_dpp(__builtin_bit_cast(int, x), CTRL, 0xf, 0xf, true));
}
constexpr int XOR1 = 0xB1, XOR2 = 0x4E, XOR7 = 0x141, XOR8 = 0x128;
__device__ __forceinline__ float silu(float x) { return x / (1.f + __expf(-x)); }

__device__ __forceinline__ float wave_sum(float v) {
  for (int o = 32; o > 0; o >>= 1) v += __shfl_xor(v, o);
  return v;
}

template <typename AF>
__device__ void gemv8(const float* a_lds, int K, const u16* W, int ldw, int n0, float* red  , float* outv) {}

__global__ void __launch_bounds__(256) k_mod(const u16* c, const u16* wmod, float* mod) {
  __shared__ float sc[NB * D];
  for (int i = threadIdx.x; i < NB * D; i += 256) sc[i] = rbf(silu(bf2f(c[i])));
  __syncthreads();
  int n = blockIdx.x * 256 + threadIdx.x;
  float acc[NB];
  for (int r = 0; r < NB; ++r) acc[r] = 0.f;
  for (int k = 0; k < D; ++k) {
    float w = bf2f(wmod[(size_t)k * 3072 + n]);
    for (int r = 0; r < NB; ++r) acc[r] = fmaf(sc[r * D + k], w, acc[r]);
  }
  for (int r = 0; r < NB; ++r) mod[r * 3072 + n] = rbf(acc[r]);
}

__device__ void ln_mod_row(const float* xrow  , const float* shift, const float* scale, float* hout, int lane) {
  float v[8];
  float s = 0.f;
  for (int i = 0; i < 8; ++i) { v[i] = xrow[lane + 64 * i]; s += v[i]; }
  s = wave_sum(s);
  float mean = s * (1.f / D);
  float q = 0.f;
  for (int i = 0; i < 8; ++i) { float d = v[i] - mean; q += d * d; }
  q = wave_sum(q);
  float var = rbf(q * (1.f / D));
  float m = rbf(mean);
  float den = rbf(sqrtf(rbf(var + 1e-5f)));
  for (int i = 0; i < 8; ++i) {
    int k = lane + 64 * i;
    float ln = rbf(rbf(v[i] - m) / den);
    hout[k] = rbf(rbf(ln * rbf(1.f + scale[k])) + shift[k]);
  }
}

__global__ void __launch_bounds__(512) k_q(const u16* x, const float* mod, const u16* wq, u16* q) {
  __shared__ float xs[NB * D];
  __shared__ float hs[NB * D];
  for (int i = threadIdx.x; i < NB * D; i += 512) xs[i] = bf2f(x[i]);
  __syncthreads();
  int w = threadIdx.x >> 6, lane = threadIdx.x & 63;
  ln_mod_row(xs + w * D, mod + w * 3072, mod + w * 3072 + 512, hs + w * D, lane);
  __syncthreads();
  int n = blockIdx.x * 512 + threadIdx.x;
  float acc[NB];
  for (int r = 0; r < NB; ++r) acc[r] = 0.f;
  for (int k = 0; k < D; ++k) {
    float wv = bf2f(wq[(size_t)k * 4096 + n]);
    for (int r = 0; r < NB; ++r) acc[r] = fmaf(hs[r * D + k], wv, acc[r]);
  }
  for (int r = 0; r < NB; ++r) q[r * 4096 + n] = f2bf(acc[r]);
}

constexpr int TL = 8;
__global__ void __launch_bounds__(512, 2) k_attn(const u16* q, const u16* kc, const u16* vc, u16* o) {
  __shared__ float sm[8][4][2];
  __shared__ float so[8][4][128];
  const int bh = blockIdx.x;
  const int tid = threadIdx.x, lane = tid & 63, wave = __builtin_amdgcn_readfirstlane(tid >> 6), r = lane >> 4, c = lane & 15;
  const u32x4 qv = *reinterpret_cast<const u32x4*>(q + (size_t)bh * DH + c * 8);
  const u32x4* kp = reinterpret_cast<const u32x4*>(kc + (size_t)bh * SK * DH) + lane;
  const u32x4* vp = reinterpret_cast<const u32x4*>(vc + (size_t)bh * SK * DH) + lane;
  constexpr int NT = SK / 32 / 8;
  constexpr float SC = 0.08838834764831845f;
  constexpr float L2E = 1.4426950408889634f;
  float m = -INFINITY, l = 0.f, acc[8];
  for (int i = 0; i < 8; ++i) acc[i] = 0.f;
  u32x4 K0[TL], V0[TL], K1[TL], V1[TL];
  auto issue = [&](u32x4* Kb, u32x4* Vb, int it) {
    const int base = (wave + 8 * it) * 512;
#pragma unroll
    for (int j = 0; j < TL; ++j) Kb[j] = __builtin_nontemporal_load(kp + base + j * 64);
#pragma unroll
    for (int j = 0; j < TL; ++j) Vb[j] = __builtin_nontemporal_load(vp + base + j * 64);
  };
  auto compute = [&](const u32x4* Kb, const u32x4* Vb) {
    float s[TL];
#pragma unroll
    for (int j = 0; j < TL; ++j) {
      float d = dot2(Kb[j][0], qv[0], 0.f);
      d = dot2(Kb[j][1], qv[1], d);
      d = dot2(Kb[j][2], qv[2], d);
      d = dot2(Kb[j][3], qv[3], d);
      d += dpp<XOR8>(d); d += dpp<XOR7>(d); d += dpp<XOR2>(d); d += dpp<XOR1>(d);
      s[j] = rbfs(rbfs(d) * SC) * L2E;
    }
    float mx = s[0];
#pragma unroll
    for (int j = 1; j < TL; ++j) mx = fmaxf(mx, s[j]);
    const float mn = fmaxf(m, mx);
    const float alpha = __builtin_amdgcn_exp2f(m - mn);
    m = mn;
    l *= alpha;
#pragma unroll
    for (int i = 0; i < 8; ++i) acc[i] *= alpha;
#pragma unroll
    for (int j = 0; j < TL; ++j) {
      const float p = __builtin_amdgcn_exp2f(s[j] - mn);
      l += p;
#pragma unroll
      for (int k = 0; k < 4; ++k) {
        acc[2 * k] = fmaf(p, lo16(Vb[j][k]), acc[2 * k]);
        acc[2 * k + 1] = fmaf(p, hi16(Vb[j][k]), acc[2 * k + 1]);
      }
    }
  };
  issue(K0, V0, 0);
  for (int it = 0; it < NT; it += 2) {
    issue(K1, V1, it + 1);
    compute(K0, V0);
    if (it + 2 < NT) issue(K0, V0, it + 2);
    compute(K1, V1);
  }
  if (c == 0) { sm[wave][r][0] = m; sm[wave][r][1] = l; }
#pragma unroll
  for (int i = 0; i < 8; ++i) so[wave][r][c * 8 + i] = acc[i];
  __syncthreads();
  if (tid < 128) {
    float M = -INFINITY;
    for (int w = 0; w < 32; ++w) M = fmaxf(M, sm[w >> 2][w & 3][0]);
    float L = 0.f, O = 0.f;
    for (int w = 0; w < 32; ++w) {
      float a = __builtin_amdgcn_exp2f(sm[w >> 2][w & 3][0] - M);
      L = fmaf(a, sm[w >> 2][w & 3][1], L);
      O = fmaf(a, so[w >> 2][w & 3][tid], O);
    }
    o[(size_t)bh * DH + tid] = f2bf(O / L);
  }
}

__global__ void __launch_bounds__(512) k_wo(const u16* o, const u16* wo, const u16* x, const float* mod, float* x1) {
  __shared__ float os[NB * 4096 / 4];
  __shared__ float red[8][NB][64];
  int col = blockIdx.x * 64 + (threadIdx.x & 63), kg = threadIdx.x >> 6;
  float acc[NB];
  for (int r = 0; r < NB; ++r) acc[r] = 0.f;
  for (int ch = 0; ch < 4; ++ch) {
    __syncthreads();
    for (int i = threadIdx.x; i < NB * 1024; i += 512) os[i] = bf2f(o[(i >> 10) * 4096 + ch * 1024 + (i & 1023)]);
    __syncthreads();
    for (int k = kg; k < 1024; k += 8) {
      float w = bf2f(wo[(size_t)(ch * 1024 + k) * D + col]);
      for (int r = 0; r < NB; ++r) acc[r] = fmaf(os[r * 1024 + k], w, acc[r]);
    }
  }
  for (int r = 0; r < NB; ++r) red[kg][r][threadIdx.x & 63] = acc[r];
  __syncthreads();
  {
    int r = threadIdx.x >> 6, cc = threadIdx.x & 63;
    float s = 0.f;
    for (int g = 0; g < 8; ++g) s += red[g][r][cc];
    int cl = blockIdx.x * 64 + cc;
    float a = rbf(s);
    x1[r * D + cl] = rbf(bf2f(x[r * D + cl]) + rbf(mod[r * 3072 + 1024 + cl] * a));
  }
}

__global__ void __launch_bounds__(512) k_ffn1(const float* x1, const float* mod, const u16* wg, const u16* wu, float* act) {
  __shared__ float hs[NB * D];
  __shared__ float red[16][2][NB][32];
  int w = threadIdx.x >> 6, lane = threadIdx.x & 63;
  ln_mod_row(x1 + w * D, mod + w * 3072 + 1536, mod + w * 3072 + 2048, hs + w * D, lane);
  __syncthreads();
  int col = blockIdx.x * 32 + (threadIdx.x & 31), kg = threadIdx.x >> 5;
  float ag[NB], au[NB];
  for (int r = 0; r < NB; ++r) ag[r] = au[r] = 0.f;
  for (int k = kg; k < D; k += 16) {
    float g = bf2f(wg[(size_t)k * F + col]), u = bf2f(wu[(size_t)k * F + col]);
    for (int r = 0; r < NB; ++r) { ag[r] = fmaf(hs[r * D + k], g, ag[r]); au[r] = fmaf(hs[r * D + k], u, au[r]); }
  }
  for (int r = 0; r < NB; ++r) { red[kg][0][r][threadIdx.x & 31] = ag[r]; red[kg][1][r][threadIdx.x & 31] = au[r]; }
  __syncthreads();
  if (threadIdx.x < 256) {
    int r = threadIdx.x >> 5, cc = threadIdx.x & 31;
    float g = 0.f, u = 0.f;
    for (int i = 0; i < 16; ++i) { g += red[i][0][r][cc]; u += red[i][1][r][cc]; }
    g = rbf(silu(rbf(g)));
    u = rbf(u);
    act[r * F + blockIdx.x * 32 + cc] = rbf(g * u);
  }
}

__global__ void __launch_bounds__(512) k_ffn2(const float* act, const u16* wd, const float* x1, const float* mod, u16* out) {
  __shared__ float as[NB * F];
  __shared__ float red[8][NB][64];
  for (int i = threadIdx.x; i < NB * F; i += 512) as[i] = act[i];
  __syncthreads();
  int col = blockIdx.x * 64 + (threadIdx.x & 63), kg = threadIdx.x >> 6;
  float acc[NB];
  for (int r = 0; r < NB; ++r) acc[r] = 0.f;
  for (int k = kg; k < F; k += 8) {
    float w = bf2f(wd[(size_t)k * D + col]);
    for (int r = 0; r < NB; ++r) acc[r] = fmaf(as[r * F + k], w, acc[r]);
  }
  for (int r = 0; r < NB; ++r) red[kg][r][threadIdx.x & 63] = acc[r];
  __syncthreads();
  {
    int r = threadIdx.x >> 6, cc = threadIdx.x & 63;
    float s = 0.f;
    for (int g = 0; g < 8; ++g) s += red[g][r][cc];
    int cl = blockIdx.x * 64 + cc;
    float a = rbf(s);
    out[r * D + cl] = f2bf(x1[r * D + cl] + rbf(mod[r * 3072 + 2560 + cl] * a));
  }
}

extern "C" void kernel_launch(void* const* d_in, const int* in_sizes, int n_in, void* d_out, int out_size, void* d_ws, size_t ws_size,
                              hipStream_t stream) {
  const u16* x = (const u16*)d_in[0];
  const u16* kc = (const u16*)d_in[1];
  const u16* vc = (const u16*)d_in[2];
  const u16* wq = (const u16*)d_in[3];
  const u16* wo = (const u16*)d_in[4];
  const u16* wg = (const u16*)d_in[6];
  const u16* wu = (const u16*)d_in[7];
  const u16* wd = (const u16*)d_in[8];
  const u16* c = (const u16*)d_in[9];
  const u16* wmod = (const u16*)d_in[10];
  char* ws = (char*)d_ws;
  float* mod = (float*)ws;
  u16* q = (u16*)(ws + 131072);
  u16* o = (u16*)(ws + 262144);
  float* x1 = (float*)(ws + 393216);
  float* act = (float*)(ws + 524288);
  k_mod<<<12, 256, 0, stream>>>(c, wmod, mod);
  k_q<<<8, 512, 0, stream>>>(x, mod, wq, q);
  k_attn<<<256, 512, 0, stream>>>(q, kc, vc, o);
  k_wo<<<8, 512, 0, stream>>>(o, wo, x, mod, x1);
  k_ffn1<<<43, 512, 0, stream>>>(x1, mod, wg, wu, act);
  k_ffn2<<<8, 512, 0, stream>>>(act, wd, x1, mod, (u16*)d_out);
}
```

```cpp
#include <hip/hip_runtime.h>
#include <hip/hip_bf16.h>
#include <stdint.h>

typedef unsigned short u16;
typedef unsigned long long u64;
using u32x4 = __attribute__((ext_vector_type(4))) unsigned;
using u32x2 = __attribute__((ext_vector_type(2))) unsigned;
using f32x4 = __attribute__((ext_vector_type(4))) float;
using f32x2 = __attribute__((ext_vector_type(2))) float;
using bf16x2 = __attribute__((ext_vector_type(2))) __bf16;
using bf16x8 = __attribute__((ext_vector_type(8))) __bf16;
using s16x4 = __attribute__((ext_vector_type(4))) short;
typedef __attribute__((address_space(1))) unsigned long long gu64;
typedef __attribute__((address_space(1))) unsigned gu32;
typedef __attribute__((address_space(3))) s16x4 lds_s16x4;

#define STATICW 1
#define BARM 0
#define PS1 8
#define NW1 1
#define KVPOS 3
#define STAMPS 0

constexpr int D = 512, SK = 4096, DH = 128, F = 1376;
constexpr unsigned WS_EPOCH = 0;
constexpr unsigned WS_MOD = 4096;
constexpr unsigned WS_X1 = WS_MOD + 8 * 1536 * 8;
constexpr unsigned WS_ACT = WS_X1 + 8 * 256 * 8;
constexpr unsigned WS_PART = WS_ACT + 44 * 128 * 8;
constexpr unsigned WS_END = WS_PART + 8 * 32 * 32 * 16 * 8;
constexpr int L_WO = 0;
constexpr int L_MRG = 131072;
constexpr int L_OPRV = L_MRG + 4224;
constexpr int L_FFNW = L_OPRV + 2048;
constexpr int L_CTL = L_FFNW + 22528;
constexpr int L_TOTAL = L_CTL + 64;
constexpr int L_H2 = L_WO;
static_assert(L_TOTAL <= 163840, "lds");
constexpr int L_REDA = L_FFNW;
constexpr int L_SC = L_REDA + 1024;
constexpr int L_H = L_SC + 2048;
constexpr int L_REDQ = L_H + 1024;
constexpr int L_REDB = L_OPRV;
constexpr int L_Q = L_REDQ + 4096;
static_assert(L_Q + 256 <= L_CTL, "scratch");
constexpr int NPAIR = 128;

constexpr unsigned SPIN_CAP = 1u << 21;

__device__ __forceinline__ float bf2f(u16 v) { return __uint_as_float(((unsigned)v) << 16); }
__device__ __forceinline__ u16 f2bf(float f) { return __builtin_bit_cast(u16, (__bf16)f); }
__device__ __forceinline__ float rbf(float f) { return bf2f(f2bf(f)); }
__device__ __forceinline__ float lo16(unsigned x) { return __uint_as_float(x << 16); }
__device__ __forceinline__ float hi16(unsigned x) { return __uint_as_float(x & 0xffff0000u); }
__device__ __forceinline__ unsigned pack2(float lo, float hi) { return (unsigned)f2bf(lo) | ((unsigned)f2bf(hi) << 16); }
__device__ __forceinline__ float dot2(unsigned k, unsigned q, float acc) {
  return __builtin_amdgcn_fdot2_f32_bf16(__builtin_bit_cast(bf16x2, k), __builtin_bit_cast(bf16x2, q), acc, false);
}
template <int CTRL> __device__ __forceinline__ float dpp(float x) {
  return __builtin_bit_cast(float, __builtin_amdgcn_mov_dpp(__builtin_bit_cast(int, x), CTRL, 0xf, 0xf, true));
}
constexpr int XOR1 = 0xB1, XOR2 = 0x4E, XOR7 = 0x141, XOR8 = 0x128, ROR4 = 0x124;
__device__ __forceinline__ float xrow_sum(float x) {
  auto s = __builtin_amdgcn_permlane16_swap(__float_as_uint(x), __float_as_uint(x), false, false);
  x = __uint_as_float(s[0]) + __uint_as_float(s[1]);
  auto t = __builtin_amdgcn_permlane32_swap(__float_as_uint(x), __float_as_uint(x), false, false);
  return __uint_as_float(t[0]) + __uint_as_float(t[1]);
}
__device__ __forceinline__ float xrow_max(float x) {
  auto s = __builtin_amdgcn_permlane16_swap(__float_as_uint(x), __float_as_uint(x), false, false);
  x = fmaxf(__uint_as_float(s[0]), __uint_as_float(s[1]));
  auto t = __builtin_amdgcn_permlane32_swap(__float_as_uint(x), __float_as_uint(x), false, false);
  return fmaxf(__uint_as_float(t[0]), __uint_as_float(t[1]));
}
__device__ __forceinline__ float row_sum16(float d) {
  d += dpp<XOR8>(d); d += dpp<XOR7>(d); d += dpp<XOR2>(d); d += dpp<XOR1>(d); return d;
}
__device__ __forceinline__ float wave_sum(float d) { return xrow_sum(row_sum16(d)); }
__device__ __forceinline__ float frcp(float x) { return __builtin_amdgcn_rcpf(x); }
__device__ __forceinline__ float silu(float x) { return x * frcp(1.f + __expf(-x)); }
__device__ __forceinline__ float silu_acc(float x) { return x / (1.f + expf(-x)); }

__device__ __forceinline__ void st_gran(char* ws, unsigned byte_off, unsigned val, unsigned tag) {
  __hip_atomic_store((gu64*)(ws + byte_off), ((u64)tag << 32) | val, __ATOMIC_RELAXED, __HIP_MEMORY_SCOPE_AGENT);
}
__device__ __forceinline__ u64 ld_gran(const char* ws, unsigned byte_off) {
  return __hip_atomic_load((gu64*)(ws + byte_off), __ATOMIC_RELAXED, __HIP_MEMORY_SCOPE_AGENT);
}
__device__ __forceinline__ u32x4 ld_gran2(__amdgpu_buffer_rsrc_t rs, unsigned byte_off) {
  return __builtin_amdgcn_raw_buffer_load_b128(rs, byte_off, 0, 16);
}
typedef const __attribute__((address_space(1))) u32x4* gv4p;
__device__ __forceinline__ u32x4 ldnt(gv4p p) { return __builtin_nontemporal_load(p); }
__device__ __forceinline__ s16x4 trread(const char* lds_addr) {
  return __builtin_amdgcn_ds_read_tr16_b64_v4i16((lds_s16x4*)lds_addr);
}
__device__ __forceinline__ bf16x8 mkfrag(s16x4 a, s16x4 b) {
  using s16x8 = __attribute__((ext_vector_type(8))) short;
  s16x8 v = {a[0], a[1], a[2], a[3], b[0], b[1], b[2], b[3]};
  return __builtin_bit_cast(bf16x8, v);
}
#define SCHED() __builtin_amdgcn_sched_barrier(0)
#define MEMBAR() asm volatile("" ::: "memory")
#define BARRIER() do { asm volatile("s_waitcnt lgkmcnt(0)" ::: "memory"); __builtin_amdgcn_s_barrier(); asm volatile("" ::: "memory"); } while (0)
__device__ __forceinline__ void glds16(const void* src, unsigned lds_dst) {
  unsigned keep;
  asm volatile("s_mov_b32 %0, m0\n\ts_mov_b32 m0, %2\n\ts_nop 0\n\tglobal_load_lds_dwordx4 %1, off\n\ts_mov_b32 m0, %0"
               : "=&s"(keep) : "v"(src), "s"(lds_dst) : "memory");
}
template <class P> __device__ __forceinline__ void glds16nt(P src, unsigned lds_dst) {
  unsigned keep;
  asm volatile("s_mov_b32 %0, m0\n\ts_mov_b32 m0, %2\n\ts_nop 0\n\tglobal_load_lds_dwordx4 %1, off nt\n\ts_mov_b32 m0, %0"
               : "=&s"(keep) : "v"(src), "s"(lds_dst) : "memory");
}
__device__ __forceinline__ void st_gran2(__amdgpu_buffer_rsrc_t rs, unsigned byte_off, unsigned v0, unsigned v1, unsigned tag) {
  __builtin_amdgcn_raw_buffer_store_b128(u32x4{v0, tag, v1, tag}, rs, byte_off, 0, 16);
}
#define STAMP(i) do {} while (0)

constexpr int TL = 4;
constexpr int UNIT_V = TL * 64;

__global__ void __launch_bounds__(512, 2)
fused_kernel(const u16* __restrict__ x, const u16* __restrict__ kc, const u16* __restrict__ vc, const u16* __restrict__ wq,
             const u16* __restrict__ wo, const u16* __restrict__ wg, const u16* __restrict__ wu, const u16* __restrict__ wd,
             const u16* __restrict__ cin, const u16* __restrict__ wmod, u16* __restrict__ out, char* ws) {
  __shared__ __attribute__((aligned(16))) char smem[L_TOTAL];
  const int tid = threadIdx.x, lane = tid & 63, wave = __builtin_amdgcn_readfirstlane(tid >> 6);
  const int g = lane >> 4, c = lane & 15;
  const unsigned lds0 = (unsigned)(uintptr_t)(__attribute__((address_space(3))) char*)smem;
  const int bid = blockIdx.x;
  const int team = bid >> 3, xc = bid & 7;
  const int b = team >> 2, h = (team & 3) * 8 + xc;
  const int bh = b * 32 + h;
  STAMP(0);
  const __amdgpu_buffer_rsrc_t rs = __builtin_amdgcn_make_buffer_rsrc(ws, 0, WS_END, 0x00020000);

  const unsigned epoch = __hip_atomic_load((gu32*)(ws + WS_EPOCH), __ATOMIC_RELAXED, __HIP_MEMORY_SCOPE_AGENT);
  const int sig = (h & 7) * 4 + (h >> 3);
  u16 cv[4]; u32x4 wa[4];
#pragma unroll
  for (int i = 0; i < 4; ++i) {
    const int row = (tid >> 2) + 128 * i;
    cv[i] = cin[b * D + row];
    wa[i] = *reinterpret_cast<const u32x4*>(wmod + (size_t)row * 3072 + sig * 32 + (tid & 3) * 8);
  }
  const u32x4 xr = *reinterpret_cast<const u32x4*>(x + b * D + lane * 8);
  const unsigned xp = *reinterpret_cast<const unsigned*>(x + b * D + (tid & 255) * 2);

  u32x4 wrm[3];
  {
    const int r0 = 64 * b + 8 * wave;
    wrm[0] = *reinterpret_cast<const u32x4*>(wq + (size_t)(r0 + (lane >> 4)) * 4096 + h * 128 + c * 8);
    wrm[1] = *reinterpret_cast<const u32x4*>(wq + (size_t)(r0 + 4 + (lane >> 4)) * 4096 + h * 128 + c * 8);
    wrm[2] = *reinterpret_cast<const u32x4*>(wmod + (size_t)(r0 + (lane >> 3)) * 3072 + 1024 + h * 64 + (lane & 7) * 8);
  }
  SCHED();
  gv4p kp = (gv4p)(kc + (size_t)bh * SK * DH) + lane;
  gv4p vp = (gv4p)(vc + (size_t)bh * SK * DH) + lane;
  asm volatile("" : "+v"(kp), "+v"(vp));
  u32x4 K0[TL], V0[TL], K1[TL], V1[TL];
#define ISSUE_K(KB, u) do { gv4p p_ = kp + (u) * UNIT_V; _Pragma("unroll") for (int j_ = 0; j_ < TL; ++j_) KB[j_] = ldnt(p_ + j_ * 64); } while (0)
#define ISSUE_V(VB, u) do { gv4p p_ = vp + (u) * UNIT_V; _Pragma("unroll") for (int j_ = 0; j_ < TL; ++j_) VB[j_] = ldnt(p_ + j_ * 64); } while (0)
  if (tid < 16) reinterpret_cast<unsigned*>(smem + L_CTL)[tid] = tid == 9 ? 16u : 0u;
  SCHED();
  u32x4 S2[8], S[16];
#define LOAD_S() do { _Pragma("unroll") for (int i = 0; i < 16; ++i) S[i] = *reinterpret_cast<const u32x4*>(wq + (size_t)((tid >> 4) * 16 + i) * 4096 + h * 128 + c * 8); } while (0)
#define LOAD_S2() do { _Pragma("unroll") for (int i = 0; i < 8; ++i) S2[i] = *reinterpret_cast<const u32x4*>(wmod + (size_t)((tid >> 3) + 64 * i) * 3072 + 1024 + h * 64 + (tid & 7) * 8); } while (0)
  auto modA = [&]() {
    float acc[8];
#pragma unroll
    for (int k = 0; k < 8; ++k) acc[k] = 0.f;
#pragma unroll
    for (int i = 0; i < 4; ++i) {
      const float s = rbf(silu_acc(bf2f(cv[i])));
      if ((tid & 3) == 0) reinterpret_cast<float*>(smem + L_SC)[(tid >> 2) + 128 * i] = s;
#pragma unroll
      for (int k = 0; k < 4; ++k) { acc[2 * k] = fmaf(s, lo16(wa[i][k]), acc[2 * k]); acc[2 * k + 1] = fmaf(s, hi16(wa[i][k]), acc[2 * k + 1]); }
    }
#pragma unroll
    for (int k = 0; k < 8; ++k) { float v = acc[k]; v += dpp<ROR4>(v); v += dpp<XOR8>(v); acc[k] = xrow_sum(v); }
    if (lane < 4) {
      float* r = reinterpret_cast<float*>(smem + L_REDA) + wave * 32 + lane * 8;
      *reinterpret_cast<f32x4*>(r) = f32x4{acc[0], acc[1], acc[2], acc[3]};
      *reinterpret_cast<f32x4*>(r + 4) = f32x4{acc[4], acc[5], acc[6], acc[7]};
    }
    asm volatile("" :: "v"(acc[0]), "v"(acc[7]));
  };
  modA();
  asm volatile("" :: "v"(epoch), "v"(wrm[0]), "v"(wrm[1]), "v"(wrm[2]));
  SCHED();
  {
    const unsigned dst = lds0 + L_WO + wave * 16384;
#pragma unroll
    for (int u = 0; u < 2; ++u) {
#pragma unroll
      for (int j = 0; j < 4; ++j) glds16nt(kp + (2 * wave + u) * UNIT_V + j * 64, dst + u * 8192 + j * 1024);
#pragma unroll
      for (int j = 0; j < 4; ++j) glds16nt(vp + (2 * wave + u) * UNIT_V + j * 64, dst + u * 8192 + 4096 + j * 1024);
    }
  }
  SCHED();
  BARRIER();
  const unsigned tag = epoch + 1;
  if (wave == 0) {
    if (tid < 16) {
      const float* r = reinterpret_cast<const float*>(smem + L_REDA) + tid * 2;
      float a0 = 0.f, a1 = 0.f;
#pragma unroll
      for (int w = 0; w < 8; ++w) { const f32x2 v = *reinterpret_cast<const f32x2*>(r + w * 32); a0 += v[0]; a1 += v[1]; }
      st_gran(ws, WS_MOD + (b * 1536 + sig * 16 + tid) * 8, pack2(a0, a1), tag);
    }
  }
  STAMP(1);
  SCHED();
  LOAD_S();
  SCHED();
  float mean_r, rden;
  {
    float v[8];
#pragma unroll
    for (int k = 0; k < 4; ++k) { v[2 * k] = lo16(xr[k]); v[2 * k + 1] = hi16(xr[k]); }
    float s = 0.f;
#pragma unroll
    for (int k = 0; k < 8; ++k) s += v[k];
    const float mean = wave_sum(s) * (1.f / D);
    float q = 0.f;
#pragma unroll
    for (int k = 0; k < 8; ++k) { const float d = v[k] - mean; q = fmaf(d, d, q); }
    const float var = rbf(wave_sum(q) * (1.f / D));
    mean_r = rbf(mean);
    rden = frcp(rbf(__builtin_amdgcn_sqrtf(rbf(var + 1e-5f))));
  }
  {
    u64 gs = 0, gc = 0;
    const unsigned o1 = WS_MOD + (b * 1536 + (tid & 255)) * 8, o2 = o1 + 256 * 8;
#pragma unroll
    for (int i = 0; i < 8; ++i)
#pragma unroll
      for (int k = 0; k < 4; ++k) {
        const unsigned w0 = S[2 * i][k], w1 = S[2 * i + 1][k];
        S[2 * i][k] = __builtin_amdgcn_perm(w1, w0, 0x05040100u);
        S[2 * i + 1][k] = __builtin_amdgcn_perm(w1, w0, 0x07060302u);
      }
#pragma unroll
    for (int i = 0; i < 16; i += 4) asm volatile("" : "+v"(S[i]), "+v"(S[i + 1]), "+v"(S[i + 2]), "+v"(S[i + 3]));
    SCHED();
    if (wave < 4) { gs = ld_gran(ws, o1); gc = ld_gran(ws, o2); }
    SCHED();
    ISSUE_K(K0, 16 + 2 * wave); ISSUE_V(V0, 16 + 2 * wave);
    SCHED();
    if (wave < 4) {
      if (!__all((unsigned)(gs >> 32) == tag && (unsigned)(gc >> 32) == tag)) {
        for (unsigned spin = 0; spin < SPIN_CAP; ++spin) {
          MEMBAR();
          gs = ld_gran(ws, o1); gc = ld_gran(ws, o2);
          if (__all((unsigned)(gs >> 32) == tag && (unsigned)(gc >> 32) == tag)) break;
          __builtin_amdgcn_s_sleep(2);
        }
      }
      const float x0 = lo16(xp), x1 = hi16(xp);
      const float h0 = rbf(rbf(rbf(x0 - mean_r) * rden) * rbf(1.f + lo16((unsigned)gc))) + lo16((unsigned)gs);
      const float h1 = rbf(rbf(rbf(x1 - mean_r) * rden) * rbf(1.f + hi16((unsigned)gc))) + hi16((unsigned)gs);
      *reinterpret_cast<unsigned*>(smem + L_H + tid * 4) = pack2(h0, h1);
    }
  }
  STAMP(2);
  BARRIER();
  STAMP(11);
  ISSUE_K(K1, 16 + 2 * wave + 1); ISSUE_V(V1, 16 + 2 * wave + 1);
  SCHED();
  u32x4 qv;
  {
    float acc[8];
#pragma unroll
    for (int k = 0; k < 8; ++k) acc[k] = 0.f;
    const u32x4 ha = *reinterpret_cast<const u32x4*>(smem + L_H + (tid >> 4) * 32);
    const u32x4 hb = *reinterpret_cast<const u32x4*>(smem + L_H + (tid >> 4) * 32 + 16);
#pragma unroll
    for (int i = 0; i < 8; ++i) {
      const unsigned hp = i < 4 ? ha[i & 3] : hb[i & 3];
#pragma unroll
      for (int k = 0; k < 4; ++k) { acc[2 * k] = dot2(S[2 * i][k], hp, acc[2 * k]); acc[2 * k + 1] = dot2(S[2 * i + 1][k], hp, acc[2 * k + 1]); }
    }
#pragma unroll
    for (int k = 0; k < 8; ++k) acc[k] = xrow_sum(acc[k]);
    if (lane < 16) {
      float* r = reinterpret_cast<float*>(smem + L_REDQ) + wave * 128 + lane * 8;
      *reinterpret_cast<f32x4*>(r) = f32x4{acc[0], acc[1], acc[2], acc[3]};
      *reinterpret_cast<f32x4*>(r + 4) = f32x4{acc[4], acc[5], acc[6], acc[7]};
    }
    STAMP(14);
    BARRIER();
    if (wave == 1) {
      const float* r = reinterpret_cast<const float*>(smem + L_REDQ) + lane * 2;
      float a0 = 0.f, a1 = 0.f;
#pragma unroll
      for (int w = 0; w < 8; ++w) { const f32x2 v = *reinterpret_cast<const f32x2*>(r + w * 128); a0 += v[0]; a1 += v[1]; }
      const unsigned qq = pack2(a0, a1);
      *reinterpret_cast<unsigned*>(smem + L_Q + lane * 4) = qq;
    }
    BARRIER();
    qv = *reinterpret_cast<const u32x4*>(smem + L_Q + c * 16);
  }
  float scv[8];
#pragma unroll
  for (int i = 0; i < 8; ++i) scv[i] = reinterpret_cast<const float*>(smem + L_SC)[(tid >> 3) + 64 * i];
  BARRIER();
  STAMP(3);
  LOAD_S2();
  SCHED();

  const bool isR2 = (bid < 176) && (xc * 22 + team < 172);
  const int p2 = xc * 22 + team;
  const bool isR3 = bid >= 192;
  const int jj = xc * 8 + ((bid - 192) >> 3);

  constexpr float SC = 0.08838834764831845f;
  constexpr float L2E = 1.4426950408889634f;
  float m = -INFINITY, l = 0.f, acc[8], s_[TL];
#pragma unroll
  for (int i = 0; i < 8; ++i) acc[i] = 0.f;
#define QK(KB) do { \
    _Pragma("unroll") for (int j_ = 0; j_ < TL; ++j_) { \
      float d_ = dot2(KB[j_][0], qv[0], 0.f); d_ = dot2(KB[j_][1], qv[1], d_); d_ = dot2(KB[j_][2], qv[2], d_); d_ = dot2(KB[j_][3], qv[3], d_); \
      d_ = row_sum16(d_); s_[j_] = rbf(rbf(d_) * SC) * L2E; } } while (0)
#define PV(VB) do { \
    float mx_ = s_[0]; \
    _Pragma("unroll") for (int j_ = 1; j_ < TL; ++j_) mx_ = fmaxf(mx_, s_[j_]); \
    const float mn_ = fmaxf(m, mx_); const float al_ = __builtin_amdgcn_exp2f(m - mn_); m = mn_; l *= al_; \
    _Pragma("unroll") for (int i_ = 0; i_ < 8; ++i_) acc[i_] *= al_; \
    _Pragma("unroll") for (int j_ = 0; j_ < TL; ++j_) { \
      const float p_ = __builtin_amdgcn_exp2f(s_[j_] - mn_); l += p_; \
      _Pragma("unroll") for (int k_ = 0; k_ < 4; ++k_) { acc[2 * k_] = fmaf(p_, lo16(VB[j_][k_]), acc[2 * k_]); acc[2 * k_ + 1] = fmaf(p_, hi16(VB[j_][k_]), acc[2 * k_ + 1]); } } \
  } while (0)
#define PIN_QK() asm volatile("" :: "v"(s_[0]), "v"(s_[1]), "v"(s_[2]), "v"(s_[3]))
#define PIN_PV() asm volatile("" :: "v"(acc[0]), "v"(acc[1]), "v"(acc[2]), "v"(acc[3]), "v"(acc[4]), "v"(acc[5]), "v"(acc[6]), "v"(acc[7]), "v"(l))
#define TOUCH(B) asm volatile("" : "+v"(B[0]), "+v"(B[1]), "+v"(B[2]), "+v"(B[3]))
#define ITER(n) do { \
    TOUCH(K0); QK(K0); PIN_QK(); SCHED(); ISSUE_K(K0, 2 * (n)); MEMBAR(); SCHED(); TOUCH(V0); PV(V0); PIN_PV(); SCHED(); ISSUE_V(V0, 2 * (n)); MEMBAR(); SCHED(); \
    TOUCH(K1); QK(K1); PIN_QK(); SCHED(); ISSUE_K(K1, 2 * (n) + 1); MEMBAR(); SCHED(); TOUCH(V1); PV(V1); PIN_PV(); SCHED(); ISSUE_V(V1, 2 * (n) + 1); MEMBAR(); SCHED(); } while (0)
#define ROWMERGE() \
    const float mw = xrow_max(m); \
    const float rs_ = __builtin_amdgcn_exp2f(m - mw); \
    const float lw = xrow_sum(l * rs_); \
    _Pragma("unroll") for (int i = 0; i < 8; ++i) acc[i] = xrow_sum(acc[i] * rs_);

  auto modB = [&]() {
    TOUCH(S2); asm volatile("" : "+v"(S2[4]), "+v"(S2[5]), "+v"(S2[6]), "+v"(S2[7]));
    float a[8];
#pragma unroll
    for (int k = 0; k < 8; ++k) a[k] = 0.f;
#pragma unroll
    for (int i = 0; i < 8; ++i) {
#pragma unroll
      for (int k = 0; k < 4; ++k) { a[2 * k] = fmaf(scv[i], lo16(S2[i][k]), a[2 * k]); a[2 * k + 1] = fmaf(scv[i], hi16(S2[i][k]), a[2 * k + 1]); }
    }
#pragma unroll
    for (int k = 0; k < 8; ++k) { float v = a[k]; v += dpp<XOR8>(v); a[k] = xrow_sum(v); }
    if (lane < 8) {
      float* r = reinterpret_cast<float*>(smem + L_REDB) + wave * 64 + lane * 8;
      *reinterpret_cast<f32x4*>(r) = f32x4{a[0], a[1], a[2], a[3]};
      *reinterpret_cast<f32x4*>(r + 4) = f32x4{a[4], a[5], a[6], a[7]};
    }
    asm volatile("s_waitcnt lgkmcnt(0)" ::: "memory");
    const int arrived = __builtin_amdgcn_readfirstlane((int)(lane == 0 ? atomicAdd(reinterpret_cast<unsigned*>(smem + L_CTL) + 10, 1u) : 0u));
    MEMBAR();
    if (arrived == 7) {
      if (lane < 32) {
        const float* r = reinterpret_cast<const float*>(smem + L_REDB) + lane * 2;
        float a0 = 0.f, a1 = 0.f;
#pragma unroll
        for (int w = 0; w < 8; ++w) { const f32x2 v = *reinterpret_cast<const f32x2*>(r + w * 64); a0 += v[0]; a1 += v[1]; }
        st_gran(ws, WS_MOD + (b * 1536 + 512 + h * 32 + lane) * 8, pack2(a0, a1), tag);
      }
    }
  };
  STAMP(4);
  auto tail_weights = [&](int nn) {
    const int j = nn - (16 + 8 * b);
    if ((unsigned)j < 8u) {
#pragma unroll
      for (int i = 0; i < 16; ++i)
        glds16(wo + (size_t)(h * 128 + j * 16 + i) * D + ((((lane >> 1) ^ (i & 7)) * 2) + (lane & 1)) * 8, lds0 + L_WO + (j * 16 + i) * 1024);
    }
    const int k = nn - (16 + 8 * (team & 7));
    if ((unsigned)k < 8u) {
      if (isR2) {
#pragma unroll
        for (int i = 0; i < 2; ++i) {
          const int row = (k * 2 + i) * 32 + (lane >> 1);
          const u16* src = ((lane & 1) ? wu : wg) + (size_t)row * F + p2 * 8;
          glds16(src, lds0 + L_FFNW + (k * 2 + i) * 1024);
        }
      } else if (isR3) {
#pragma unroll
        for (int i = 0; i < 3; ++i) {
          const int blk = k + 8 * i;
          if (blk < 22) {
            int row = blk * 64 + lane; row = row < F ? row : F - 1;
            glds16(wd + (size_t)row * D + jj * 8, lds0 + L_FFNW + blk * 1024);
          }
        }
      }
    }
  };

  float* rec = reinterpret_cast<float*>(smem + L_MRG) + wave * 132;
  {
    {
      const char* base = smem + L_WO + wave * 16384 + lane * 16;
#pragma unroll
      for (int u = 0; u < 2; ++u) {
        u32x4 T[TL], T2[TL];
#pragma unroll
        for (int j = 0; j < TL; ++j) { T[j] = *reinterpret_cast<const u32x4*>(base + u * 8192 + j * 1024); T2[j] = *reinterpret_cast<const u32x4*>(base + u * 8192 + 4096 + j * 1024); }
        QK(T); PV(T2);
      }
      PIN_PV();
      asm volatile("s_waitcnt lgkmcnt(0)" ::: "memory");
    }
    int n = 16 + wave;
    for (int it = 0; it < 13; ++it) {
      tail_weights(n);
      SCHED();
      ITER(n);
      n += 8;
      if (it == 0) modB();
      __builtin_amdgcn_s_barrier();
    }
    STAMP(10);
    ITER(n);
  }
  bf16x8 bw[16];
  {
    const int q = c >> 2, p = c & 3, r7 = (4 * g + q) & 7;
#pragma unroll
    for (int t = 0; t < 4; ++t) {
      const char* base = smem + L_WO + (4 * g + q) * 1024 + (((4 * wave + t) ^ r7) * 32) + 8 * p;
#pragma unroll
      for (int s = 0; s < 4; ++s) bw[t * 4 + s] = mkfrag(trread(base + s * 32768), trread(base + s * 32768 + 16384));
    }
#pragma unroll
    for (int i = 0; i < 16; i += 4) asm volatile("" : "+v"(bw[i]), "+v"(bw[i + 1]), "+v"(bw[i + 2]), "+v"(bw[i + 3]));
  }
  STAMP(12);
  u32x4 t0 = {0u, 0u, 0u, 0u}, t1 = t0, t2 = t0, t3 = t0;
  u64 g_a = 0, g_b = 0; unsigned x_pair = 0;
  {
    if (isR2) {
      t0 = ld_gran2(rs, WS_MOD + (wave * 1536 + 1024 + lane * 4) * 8);
      t1 = ld_gran2(rs, WS_MOD + (wave * 1536 + 1024 + lane * 4 + 2) * 8);
      t2 = ld_gran2(rs, WS_MOD + (wave * 1536 + 768 + lane * 4) * 8);
      t3 = ld_gran2(rs, WS_MOD + (wave * 1536 + 768 + lane * 4 + 2) * 8);
    }
    if (wave < NW1) {
      g_a = ld_gran(ws, WS_MOD + (b * 1536 + 512 + h * 8 + (lane & 7)) * 8);
      x_pair = *reinterpret_cast<const unsigned*>(x + b * D + h * 16 + (lane & 7) * 2);
    }
    if (isR3 && wave == 1) g_b = ld_gran(ws, WS_MOD + ((lane >> 3) * 1536 + 1280 + jj * 4 + ((lane & 7) >> 1)) * 8);
  }
  SCHED();
  TOUCH(K0); QK(K0); TOUCH(V0); PV(V0); TOUCH(K1); QK(K1); TOUCH(V1); PV(V1);
  STAMP(5);
  {
    ROWMERGE();
    if (lane < 16) {
      *reinterpret_cast<f32x4*>(rec + 4 + c * 8) = f32x4{acc[0], acc[1], acc[2], acc[3]};
      *reinterpret_cast<f32x4*>(rec + 4 + c * 8 + 4) = f32x4{acc[4], acc[5], acc[6], acc[7]};
      if (lane == 0) *reinterpret_cast<f32x2*>(rec) = f32x2{mw, lw};
    }
  }
  BARRIER();
  STAMP(15);
  {
    const float* mr = reinterpret_cast<const float*>(smem + L_MRG);
    float mv[8], lv[8], M = -INFINITY;
#pragma unroll
    for (int w = 0; w < 8; ++w) { const f32x2 v = *reinterpret_cast<const f32x2*>(mr + w * 132); mv[w] = v[0]; lv[w] = v[1]; M = fmaxf(M, v[0]); }
    float L = 0.f, o0 = 0.f, o1 = 0.f;
#pragma unroll
    for (int w = 0; w < 8; ++w) {
      const float a = __builtin_amdgcn_exp2f(mv[w] - M);
      const f32x2 v = *reinterpret_cast<const f32x2*>(mr + w * 132 + 4 + lane * 2);
      L = fmaf(a, lv[w], L); o0 = fmaf(a, v[0], o0); o1 = fmaf(a, v[1], o1);
    }
    const float inv = frcp(L);
    *reinterpret_cast<unsigned*>(smem + L_OPRV + wave * 256 + lane * 4) = pack2(o0 * inv, o1 * inv);
  }
  {
    f32x4 dacc[4];
#pragma unroll
    for (int t = 0; t < 4; ++t) dacc[t] = f32x4{0.f, 0.f, 0.f, 0.f};
#pragma unroll
    for (int s = 0; s < 4; ++s) {
      const char* ob = smem + L_OPRV + wave * 256 + (32 * s + 4 * g) * 2;
      const s16x4 a0 = *reinterpret_cast<const s16x4*>(ob), a1 = *reinterpret_cast<const s16x4*>(ob + 32);
      const bf16x8 af = mkfrag(a0, a1);
#pragma unroll
      for (int t = 0; t < 4; ++t) dacc[t] = __builtin_amdgcn_mfma_f32_16x16x32_bf16(af, bw[t * 4 + s], dacc[t], 0, 0, 0);
    }
    const float val = g == 0 ? dacc[0][0] : g == 1 ? dacc[1][0] : g == 2 ? dacc[2][0] : dacc[3][0];
    st_gran(ws, WS_PART + ((((b * 32 + 4 * wave + g) * 32) + h) * 16 + c) * 8, __float_as_uint(val), tag);
  }
  STAMP(6);

  if (wave < NW1) {
    volatile unsigned* r1done = reinterpret_cast<volatile unsigned*>(smem + L_CTL) + 11;
    const unsigned base = WS_PART + (((b * 32 + h) * 32 + (lane >> 3)) * 16 + (lane & 7) * 2) * 8;
    for (int k = 0; k < wave; ++k) __builtin_amdgcn_s_sleep(PS1);
    u32x4 pv[4];
#pragma unroll
    for (int i = 0; i < 4; ++i) pv[i] = ld_gran2(rs, base + i * 8 * 16 * 8);
    bool mine = false;
    for (unsigned spin = 0; spin < SPIN_CAP; ++spin) {
      bool ok = true;
#pragma unroll
      for (int i = 0; i < 4; ++i) ok = ok && pv[i][1] == tag && pv[i][3] == tag;
      if (__all(ok)) { mine = true; break; }
      if (__builtin_amdgcn_readfirstlane(*r1done)) break;
      MEMBAR();
#pragma unroll
      for (int i = 0; i < 4; ++i) if (!(pv[i][1] == tag && pv[i][3] == tag)) pv[i] = ld_gran2(rs, base + i * 8 * 16 * 8);
    }
    if (mine) {
      float s0 = 0.f, s1 = 0.f;
#pragma unroll
      for (int i = 0; i < 4; ++i) { s0 += __uint_as_float(pv[i][0]); s1 += __uint_as_float(pv[i][2]); }
      s0 += dpp<XOR8>(s0); s1 += dpp<XOR8>(s1);
      s0 = xrow_sum(s0); s1 = xrow_sum(s1);
      for (unsigned spin = 0; spin < SPIN_CAP; ++spin) {
        if (__all((unsigned)(g_a >> 32) == tag)) break;
        MEMBAR();
        g_a = ld_gran(ws, WS_MOD + (b * 1536 + 512 + h * 8 + (lane & 7)) * 8);
      }
      const float y0 = rbf(lo16(x_pair) + rbf(lo16((unsigned)g_a) * rbf(s0)));
      const float y1 = rbf(hi16(x_pair) + rbf(hi16((unsigned)g_a) * rbf(s1)));
      if (lane < 8) st_gran(ws, WS_X1 + (b * 256 + h * 8 + lane) * 8, pack2(y0, y1), tag);
      if (lane == 0) *r1done = 1u;
    }
    STAMP(7);
  }
  if (!isR2 && !isR3) goto done;
  BARRIER();

  if (isR2) {
    bf16x8 bf[2];
    {
      const int q = c >> 2, p = c & 3;
#pragma unroll
      for (int ss = 0; ss < 2; ++ss) {
        const char* base = smem + L_FFNW + (64 * wave + 32 * ss + 8 * g + q) * 32 + 8 * p;
        bf[ss] = mkfrag(trread(base), trread(base + 128));
      }
    }
    for (unsigned spin = 0; spin < SPIN_CAP; ++spin) {
      const bool ok = t0[1] == tag && t0[3] == tag && t1[1] == tag && t1[3] == tag && t2[1] == tag && t2[3] == tag && t3[1] == tag && t3[3] == tag;
      if (__all(ok)) break;
      MEMBAR();
      t0 = ld_gran2(rs, WS_MOD + (wave * 1536 + 1024 + lane * 4) * 8);
      t1 = ld_gran2(rs, WS_MOD + (wave * 1536 + 1024 + lane * 4 + 2) * 8);
      t2 = ld_gran2(rs, WS_MOD + (wave * 1536 + 768 + lane * 4) * 8);
      t3 = ld_gran2(rs, WS_MOD + (wave * 1536 + 768 + lane * 4 + 2) * 8);
    }
    u32x4 xa, xb;
    for (unsigned spin = 0; spin < SPIN_CAP; ++spin) {
      MEMBAR();
      xa = ld_gran2(rs, WS_X1 + (wave * 256 + lane * 4) * 8);
      xb = ld_gran2(rs, WS_X1 + (wave * 256 + lane * 4 + 2) * 8);
      const bool ok = xa[1] == tag && xa[3] == tag && xb[1] == tag && xb[3] == tag;
      if (__all(ok)) break;
      __builtin_amdgcn_s_sleep(1);
    }
    STAMP(8);
    const unsigned xw[4] = {xa[0], xa[2], xb[0], xb[2]};
    const unsigned cw[4] = {t0[0], t0[2], t1[0], t1[2]};
    const unsigned sw[4] = {t2[0], t2[2], t3[0], t3[2]};
    float v[8];
#pragma unroll
    for (int k = 0; k < 4; ++k) { v[2 * k] = lo16(xw[k]); v[2 * k + 1] = hi16(xw[k]); }
    float s = 0.f;
#pragma unroll
    for (int k = 0; k < 8; ++k) s += v[k];
    const float mean = wave_sum(s) * (1.f / D);
    float qq = 0.f;
#pragma unroll
    for (int k = 0; k < 8; ++k) { const float d = v[k] - mean; qq = fmaf(d, d, qq); }
    const float var = rbf(wave_sum(qq) * (1.f / D));
    const float mr = rbf(mean), rd2 = frcp(rbf(__builtin_amdgcn_sqrtf(rbf(var + 1e-5f))));
    u32x4 hv;
#pragma unroll
    for (int k = 0; k < 4; ++k) {
      const float h0 = rbf(rbf(rbf(v[2 * k] - mr) * rd2) * rbf(1.f + lo16(cw[k]))) + lo16(sw[k]);
      const float h1 = rbf(rbf(rbf(v[2 * k + 1] - mr) * rd2) * rbf(1.f + hi16(cw[k]))) + hi16(sw[k]);
      hv[k] = pack2(h0, h1);
    }
    *reinterpret_cast<u32x4*>(smem + L_H2 + wave * 1040 + lane * 16) = hv;
    __syncthreads();
    f32x4 dd = {0.f, 0.f, 0.f, 0.f};
#pragma unroll
    for (int ss = 0; ss < 2; ++ss) {
      const bf16x8 af = *reinterpret_cast<const bf16x8*>(smem + L_H2 + (c & 7) * 1040 + (64 * wave + 32 * ss + 8 * g) * 2);
      dd = __builtin_amdgcn_mfma_f32_16x16x32_bf16(af, bf[ss], dd, 0, 0, 0);
    }
    float* red = reinterpret_cast<float*>(smem + L_MRG);
    if (g < 2) {
#pragma unroll
      for (int i = 0; i < 4; ++i) red[wave * 128 + (4 * g + i) * 16 + c] = dd[i];
    }
    __syncthreads();
    if (tid < 64) {
      const int row = tid >> 3, f = tid & 7;
      float gg = 0.f, uu = 0.f;
#pragma unroll
      for (int w = 0; w < 8; ++w) { gg += red[w * 128 + row * 16 + f]; uu += red[w * 128 + row * 16 + 8 + f]; }
      const float a = rbf(rbf(silu(rbf(gg))) * rbf(uu));
      const float an = dpp<XOR1>(a);
      if ((f & 1) == 0) st_gran(ws, WS_ACT + ((p2 * 8 + row) * 4 + (f >> 1)) * 8, pack2(a, an), tag);
    }
    STAMP(9);
  }

  if (isR3) {
    bf16x8 bf[3];
    {
      const int q = c >> 2, p = c & 3;
#pragma unroll
      for (int i = 0; i < 3; ++i) {
        const int mm = wave + 8 * i;
        const int mc = mm < 22 ? mm : 21;
        const char* base = smem + L_FFNW + (64 * mc + 32 * (p >> 1) + 8 * g + q) * 16 + (p & 1) * 8;
        bf[i] = mkfrag(trread(base), trread(base + 64));
      }
    }
    u32x4 aa[3], ab[3];
    u64 x1g = 0;
    for (unsigned spin = 0; spin < SPIN_CAP; ++spin) {
      MEMBAR();
      bool ok = true;
#pragma unroll
      for (int i = 0; i < 3; ++i) {
        const int ks = 2 * (wave + 8 * i) + (c >> 3);
        const unsigned off = WS_ACT + (((ks * 4 + g) * 8 + (c & 7)) * 4) * 8;
        if (ks < 43) {
          aa[i] = ld_gran2(rs, off); ab[i] = ld_gran2(rs, off + 16);
        } else { aa[i] = u32x4{0u, tag, 0u, tag}; ab[i] = aa[i]; }
      }
      if (wave == 1) x1g = ld_gran(ws, WS_X1 + ((lane >> 3) * 256 + jj * 4 + ((lane & 7) >> 1)) * 8);
#pragma unroll
      for (int i = 0; i < 3; ++i) ok = ok && aa[i][1] == tag && aa[i][3] == tag && ab[i][1] == tag && ab[i][3] == tag;
      if (wave == 1) ok = ok && (unsigned)(x1g >> 32) == tag;
      if (__all(ok)) break;
      __builtin_amdgcn_s_sleep(1);
    }
    f32x4 dd = {0.f, 0.f, 0.f, 0.f};
#pragma unroll
    for (int i = 0; i < 3; ++i) {
      const u32x4 fa = {aa[i][0], aa[i][2], ab[i][0], ab[i][2]};
      f32x4 r = __builtin_amdgcn_mfma_f32_16x16x32_bf16(__builtin_bit_cast(bf16x8, fa), bf[i], dd, 0, 0, 0);
      if (wave + 8 * i < 22) dd = r;
    }
    float* red = reinterpret_cast<float*>(smem + L_MRG);
    if ((c >> 3) == (g >> 1)) {
#pragma unroll
      for (int i = 0; i < 4; ++i) red[wave * 128 + (g >> 1) * 64 + (4 * (g & 1) + i) * 8 + (c & 7)] = dd[i];
    }
    __syncthreads();
    if (wave == 1) {
      float s = 0.f;
#pragma unroll
      for (int w = 0; w < 16; ++w) s += red[w * 64 + lane];
      for (unsigned spin = 0; spin < SPIN_CAP; ++spin) {
        if (__all((unsigned)(g_b >> 32) == tag)) break;
        MEMBAR();
        g_b = ld_gran(ws, WS_MOD + ((lane >> 3) * 1536 + 1280 + jj * 4 + ((lane & 7) >> 1)) * 8);
      }
      const bool hi = lane & 1;
      const float xv = hi ? hi16((unsigned)x1g) : lo16((unsigned)x1g);
      const float gv = hi ? hi16((unsigned)g_b) : lo16((unsigned)g_b);
      const float y = xv + rbf(gv * rbf(s));
      const float yn = dpp<XOR1>(y);
      if (!hi) *reinterpret_cast<unsigned*>(out + (lane >> 3) * D + jj * 8 + (lane & 7)) = pack2(y, yn);
      if (bid == 192 && lane == 0) __hip_atomic_store((gu32*)(ws + WS_EPOCH), tag, __ATOMIC_RELAXED, __HIP_MEMORY_SCOPE_AGENT);
    }
    STAMP(13);
  }
done:;
}

extern "C" void kernel_launch(void* const* d_in, const int* in_sizes, int n_in, void* d_out, int out_size, void* d_ws, size_t ws_size,
                              hipStream_t stream) {
  fused_kernel<<<256, 512, 0, stream>>>((const u16*)d_in[0], (const u16*)d_in[1], (const u16*)d_in[2], (const u16*)d_in[3],
                                        (const u16*)d_in[4], (const u16*)d_in[6], (const u16*)d_in[7], (const u16*)d_in[8],
                                        (const u16*)d_in[9], (const u16*)d_in[10], (u16*)d_out, (char*)d_ws);
}
```
